```python
import torch

def _softmax(x, axis=-1):
    return torch.softmax(x, dim=axis)
def _gelu(x):
    return torch.nn.functional.gelu(x, approximate="tanh")
def _silu(x):
    return torch.nn.functional.silu(x)
def _sigmoid(x):
    return torch.sigmoid(x)
def _layernorm(x, axis=-1, eps=1e-5):
    m = x.mean(dim=axis, keepdim=True)
    v = x.var(dim=axis, keepdim=True, unbiased=False)
    return (x - m) / torch.sqrt(v + eps)
def _scatter_add(tgt, idx, src, axis=0):
    return tgt.clone().index_add_(axis, idx, src)
def _segment_sum(src, seg, n):
    out = torch.zeros((n,) + src.shape[1:], dtype=src.dtype)
    return out.index_add_(0, seg, src)
def _pad(x, pw):
    flat = tuple(v for pair in reversed(pw) for v in pair)
    return torch.nn.functional.pad(x, flat)

kernel_name = 'parallel_sum__ssm_conv_h3__mixer_token_mlp__prompt'

def setup_inputs():

    _w = (lambda a: (lambda t: t.to(torch.bfloat16) if t.is_floating_point() else t)(torch.as_tensor(a)))
    def _A_setup():
        B, S, D = 1, 2048, 1024
        ks = 4
        return {
            "q": _w((lambda *s: torch.randn(*s, dtype=torch.bfloat16) * 0.05)(B, S, D)),
            "k": _w((lambda *s: torch.randn(*s, dtype=torch.bfloat16) * 0.2)(B, S, D)),
            "v": _w((lambda *s: torch.randn(*s, dtype=torch.bfloat16) * 0.2)(B, S, D)),
            "shift_k": _w((lambda *s: torch.randn(*s, dtype=torch.bfloat16) * 0.2)(D, ks) * 0.5),
            "ssm_k": _w((lambda *s: torch.randn(*s, dtype=torch.bfloat16) * 0.2)(D, S) * (1.0 / S ** 0.5)),
            "ssm_D": _w((lambda *s: torch.randn(*s, dtype=torch.bfloat16) * 0.2)(D)),
        }
    _A_in = _A_setup()

    def _B_setup():
        B, S, D, F = 1, 2048, 1024, 3584
        return {
            "x": _w((lambda *s: torch.randn(*s, dtype=torch.bfloat16) * 0.05)(B, S, D)),
            "w1": _w((lambda *s: torch.randn(*s, dtype=torch.bfloat16) * 0.4)(S, F) * (S ** -0.5)),
            "b1": _w((lambda *s: torch.randn(*s, dtype=torch.bfloat16) * 3.2)(F) * 0.02),
            "w2": _w((lambda *s: torch.randn(*s, dtype=torch.bfloat16) * 0.2)(F, S) * (F ** -0.5)),
            "b2": _w((lambda *s: torch.randn(*s, dtype=torch.bfloat16) * 0.4)(S) * 0.02),
        }
    _B_in = _B_setup()

    return {
        "A_q": _A_in["q"],
        "A_k": _A_in["k"],
        "A_v": _A_in["v"],
        "A_shift_k": _A_in["shift_k"],
        "A_ssm_k": _A_in["ssm_k"],
        "A_ssm_D": _A_in["ssm_D"],
        "B_x": _B_in["x"],
        "B_w1": _B_in["w1"],
        "B_b1": _B_in["b1"],
        "B_w2": _B_in["w2"],
        "B_b2": _B_in["b2"],
    }

def reference(A_q, A_k, A_v, A_shift_k, A_ssm_k, A_ssm_D, B_x, B_w1, B_b1, B_w2, B_b2):
    def _A_ref(q, k, v, shift_k, ssm_k, ssm_D):
        B, S, D = q.shape
        ks = shift_k.shape[-1]
        k_pad = _pad(k, ((0, 0), (ks - 1, 0), (0, 0)))
        k_shift = torch.zeros_like(k)
        for t in range(ks):
            k_shift = k_shift + k_pad[:, t:t + S, :] * shift_k[:, ks - 1 - t][None, None, :]
        kv = k_shift * v
        L = 2 * S
        kv_pad = _pad(kv, ((0, 0), (0, S), (0, 0)))
        kf_pad = _pad(ssm_k, ((0, 0), (0, S)))
        kv_f = (lambda x, axes=(-2,-1): torch.fft.rfft2(x.float(), dim=axes))(kv_pad[:, None, :, :], axes=(1, 2))
        kf_f = (lambda x, axes=(-2,-1): torch.fft.rfft2(x.float(), dim=axes))(kf_pad[:, None, :], axes=(1, 2))
        kf_f = (lambda x, *d: x.permute(*d))(kf_f, 1, 2, 0)
        y_f = kv_f * kf_f[None, :, :, :]
        y = (lambda x, s, axes=(-2,-1): torch.fft.irfft2(x, s=s, dim=axes).to(torch.bfloat16))(y_f, (1, L), axes=(1, 2))[:, 0, :S, :]
        y = y + kv * ssm_D[None, None, :]
        return q * y

    def _B_ref(x, w1, b1, w2, b2):
        B, S, D = x.shape
        h = _layernorm(x, axis=-1)
        u = torch.einsum("bsd,sf->bdf", h, w1) + b1
        u = _gelu(u)
        y = torch.einsum("bdf,fs->bds", u, w2) + b2
        return (lambda x, *d: x.permute(*d))(y, 0, 2, 1)

    x = A_q
    xn = _layernorm(x, axis=-1)
    return x + _A_ref(xn, A_k, A_v, A_shift_k, A_ssm_k, A_ssm_D) + _B_ref(xn, B_w1, B_b1, B_w2, B_b2)
```

```cpp
#include <hip/hip_runtime.h>
#include <hip/hip_bf16.h>
#include <stdint.h>

typedef unsigned short u16;
typedef unsigned int u32;
typedef __attribute__((ext_vector_type(4))) short s16x4;
typedef __attribute__((ext_vector_type(8))) short s16x8;
typedef __attribute__((ext_vector_type(8))) __bf16 bf16x8;
typedef __attribute__((ext_vector_type(2))) __bf16 bf16x2;
typedef __attribute__((ext_vector_type(4))) float f32x4;
typedef __attribute__((ext_vector_type(2))) float f32x2;
typedef __attribute__((ext_vector_type(16))) float f32x16;
typedef __attribute__((ext_vector_type(4))) unsigned u32x4;
typedef __attribute__((ext_vector_type(2))) unsigned u32x2;
typedef __attribute__((address_space(3))) s16x4* lds_s16x4p;
typedef __attribute__((address_space(3))) u32x4* lds_u32x4p;
typedef __attribute__((address_space(3))) u32x2* lds_u32x2p;
typedef __attribute__((address_space(3))) char* lds_charp;
typedef __attribute__((address_space(1))) unsigned gu32;

static constexpr int S = 2048, D = 1024, F = 3584;

static constexpr size_t WS_HFRAG = 0;
static constexpr size_t WS_KVT = WS_HFRAG + (size_t)S * D * 2;
static constexpr size_t WS_YP8 = WS_KVT + (size_t)S * D * 2;
static constexpr size_t WS_UFRAG = WS_YP8 + (size_t)S * D * 2;
static constexpr size_t WS_PART = WS_UFRAG + (size_t)D * F * 2;
static constexpr size_t WS_STATS = WS_PART + (size_t)S * D * 4;
static constexpr size_t WS_FLAGS = WS_STATS + (size_t)S * 8;
static constexpr int FLAG_STRIDE = 32;

__device__ __forceinline__ float bf2f(u16 v) { return __uint_as_float(((u32)v) << 16); }
__device__ __forceinline__ float bflo(u32 v) { return __uint_as_float(v << 16); }
__device__ __forceinline__ float bfhi(u32 v) { return __uint_as_float(v & 0xffff0000u); }
__device__ __forceinline__ u32 pack2(float a, float b) {
  f32x2 v = {a, b};
  bf16x2 r = __builtin_convertvector(v, bf16x2);
  return __builtin_bit_cast(u32, r);
}
__device__ __forceinline__ float rbf(float f) { return bflo(pack2(f, f)); }
__device__ __forceinline__ u32 rfl(u32 x) { return __builtin_amdgcn_readfirstlane(x); }
__device__ __forceinline__ __amdgpu_buffer_rsrc_t make_srd(const void* p, u32 bytes) {
  return __builtin_amdgcn_make_buffer_rsrc((void*)p, (short)0, (int)bytes, 0x00020000);
}
template <int IMM>
__device__ __forceinline__ void ldfrag(bf16x8& dst, u32 voff, __amdgpu_buffer_rsrc_t srd, u32 soff) {
  asm volatile("buffer_load_dwordx4 %0, %1, %2, %3 offen offset:%4"
               : "+v"(dst) : "v"(voff), "s"(srd), "s"(soff), "i"(IMM) : "memory");
}
template <int IMM>
__device__ __forceinline__ void dma16i(u32 voff, __amdgpu_buffer_rsrc_t srd, u32 soff) {
  asm volatile("buffer_load_dwordx4 %0, %1, %2 offen offset:%3 lds" :: "v"(voff), "s"(srd), "s"(soff), "i"(IMM) : "memory");
}
__device__ __forceinline__ void set_m0(u32 v) { asm volatile("s_mov_b32 m0, %0" :: "s"(v) : "memory"); }
template <int VM, int LGKM>
__device__ __forceinline__ void waitcnt() {
  __builtin_amdgcn_s_waitcnt((VM & 15) | (7 << 4) | (LGKM << 8) | ((VM >> 4) << 14));
}
__device__ __forceinline__ void touch(u32& sink, u32 voff, __amdgpu_buffer_rsrc_t srd, u32 soff) {
  asm volatile("s_nop 4\n\tbuffer_load_dword %0, %1, %2, %3 offen" : "+v"(sink) : "v"(voff), "s"(srd), "s"(rfl(soff)) : "memory");
}
__device__ __forceinline__ bf16x8 tr_frag(u32 a0, u32 a1) {
  s16x4 lo = __builtin_amdgcn_ds_read_tr16_b64_v4i16((lds_s16x4p)(size_t)a0);
  s16x4 hi = __builtin_amdgcn_ds_read_tr16_b64_v4i16((lds_s16x4p)(size_t)a1);
  s16x8 r = __builtin_shufflevector(lo, hi, 0, 1, 2, 3, 4, 5, 6, 7);
  return __builtin_bit_cast(bf16x8, r);
}
template <int CTRL, int RM, int BM>
__device__ __forceinline__ float dpp_add(float v) {
  int o = __builtin_amdgcn_update_dpp(0, __float_as_int(v), CTRL, RM, BM, true);
  return v + __int_as_float(o);
}
__device__ __forceinline__ float wave_sum(float v) {
  v = dpp_add<0x111, 0xf, 0xf>(v);
  v = dpp_add<0x112, 0xf, 0xf>(v);
  v = dpp_add<0x114, 0xf, 0xf>(v);
  v = dpp_add<0x118, 0xf, 0xf>(v);
  v = dpp_add<0x142, 0xa, 0xf>(v);
  v = dpp_add<0x143, 0xc, 0xf>(v);
  return __int_as_float(__builtin_amdgcn_readlane(__float_as_int(v), 63));
}

static constexpr int LN_RS = 2048 + 64;
#define PF0_WAVES 256

__global__ __launch_bounds__(768) void k_prep(const u16* __restrict__ x, const u16* __restrict__ w1,
                                              u16* __restrict__ hfrag, float2* __restrict__ stats,
                                              u32* __restrict__ flags, const u16* __restrict__ kin,
                                              const u16* __restrict__ vin, const u16* __restrict__ shk,
                                              u16* __restrict__ kvT) {
  __shared__ __attribute__((aligned(16))) char sm[8 * LN_RS];
  const int tid = threadIdx.x, lane = tid & 63, w = rfl(tid >> 6);

  if (w >= 8) {
    const int gw = blockIdx.x * 4 + (w - 8);
    {
      const int st = gw >> 5, dt = gw & 31;
      const int sblk = lane >> 3, dch = lane & 7;
      const int s0 = st * 64 + sblk * 8, d0 = dt * 32 + dch * 4;
      u32x2 kr[11], vr[8];
#pragma unroll
      for (int i = 0; i < 11; i++) {
        const int ss = s0 - 3 + i;
        kr[i] = *(const u32x2*)(kin + (size_t)(ss < 0 ? 0 : ss) * D + d0);
      }
#pragma unroll
      for (int i = 0; i < 8; i++)
        vr[i] = *(const u32x2*)(vin + (size_t)(s0 + i) * D + d0);
      u32x4 sh[2];
#pragma unroll
      for (int i = 0; i < 2; i++) sh[i] = *(const u32x4*)(shk + (size_t)d0 * 4 + i * 8);
      __builtin_amdgcn_sched_barrier(0);
      __builtin_amdgcn_s_barrier();
      __builtin_amdgcn_sched_barrier(0);
#pragma unroll
      for (int i = 0; i < 3; i++) { kr[i][0] = s0 == 0 ? 0u : kr[i][0]; kr[i][1] = s0 == 0 ? 0u : kr[i][1]; }
#pragma unroll
      for (int dd = 0; dd < 4; dd++) {
        const u32 s01 = sh[dd >> 1][(dd & 1) * 2], s23 = sh[dd >> 1][(dd & 1) * 2 + 1];
        const float c0 = bflo(s01), c1 = bfhi(s01), c2 = bflo(s23), c3 = bfhi(s23);
        float kk[11];
#pragma unroll
        for (int i = 0; i < 11; i++) kk[i] = (dd & 1) ? bfhi(kr[i][dd >> 1]) : bflo(kr[i][dd >> 1]);
        float o[8];
#pragma unroll
        for (int i = 0; i < 8; i++) {
          float acc = kk[i] * c3;
          acc = __builtin_fmaf(kk[i + 1], c2, acc);
          acc = __builtin_fmaf(kk[i + 2], c1, acc);
          acc = __builtin_fmaf(kk[i + 3], c0, acc);
          o[i] = acc * ((dd & 1) ? bfhi(vr[i][dd >> 1]) : bflo(vr[i][dd >> 1]));
        }
        u32x4 ov;
#pragma unroll
        for (int i = 0; i < 4; i++) ov[i] = pack2(o[2 * i], o[2 * i + 1]);
        *(u32x4*)(kvT + (size_t)(d0 + dd) * S + s0) = ov;
      }
    }

    return;
  }
  const int k16 = blockIdx.x >> 1, hh = blockIdx.x & 1;
  const int s = 16 * k16 + 4 * hh + (w & 3) + 8 * (w >> 2);
  u32x4 a, b;
  u32 sink = 0;
  {
    __amdgpu_buffer_rsrc_t srdx = make_srd(x, (u32)S * D * 2);
    const u32 vo = (u32)s * (D * 2) + lane * 16;
    asm volatile("buffer_load_dwordx4 %0, %2, %3, 0 offen\n\tbuffer_load_dwordx4 %1, %2, %3, 0 offen offset:1024"
                 : "=&v"(a), "=&v"(b) : "v"(vo), "s"(srdx) : "memory");
    touch(sink, lane * 128 + (w * 256 + blockIdx.x) * 8192, make_srd(w1, PF0_WAVES * 8192u), 0u);
    asm volatile("s_waitcnt vmcnt(1)" : "+v"(a), "+v"(b) :: "memory");
  }
  if (tid < 4) flags[(blockIdx.x * 4 + tid) * FLAG_STRIDE] = 0u;
  float v[16];
#pragma unroll
  for (int i = 0; i < 4; i++) { v[2 * i] = bflo(a[i]); v[2 * i + 1] = bfhi(a[i]); v[8 + 2 * i] = bflo(b[i]); v[9 + 2 * i] = bfhi(b[i]); }
#pragma unroll
  for (int pass = 0; pass < 2; pass++) {
    float sum = 0;
#pragma unroll
    for (int i = 0; i < 16; i++) sum += v[i];
    float mean = wave_sum(sum) * (1.f / D);
    float sq = 0;
#pragma unroll
    for (int i = 0; i < 16; i++) { float t = v[i] - mean; sq += t * t; }
    float var = wave_sum(sq) * (1.f / D);
    float m = rbf(mean);
    float den = rbf(sqrtf(rbf(rbf(var) + 1e-5f)));
    float rinv = 1.f / den;
    if (pass == 0 && lane == 0) stats[s] = make_float2(m, rinv);
#pragma unroll
    for (int i = 0; i < 8; i++) {
      u32 t = pack2(v[2 * i] - m, v[2 * i + 1] - m);
      t = pack2(bflo(t) * rinv, bfhi(t) * rinv);
      v[2 * i] = bflo(t); v[2 * i + 1] = bfhi(t);
    }
  }
  u32x4 oa, ob;
#pragma unroll
  for (int i = 0; i < 4; i++) { oa[i] = pack2(v[2 * i], v[2 * i + 1]); ob[i] = pack2(v[8 + 2 * i], v[9 + 2 * i]); }
  *(u32x4*)(sm + w * LN_RS + lane * 16) = oa;
  *(u32x4*)(sm + w * LN_RS + 1024 + lane * 16) = ob;

  __syncthreads();
  const int grp = lane >> 4, q = (lane >> 2) & 3, p = lane & 3;
  const u32 smb = (u32)(size_t)(lds_charp)sm;
#pragma unroll
  for (int it = 0; it < 2; it++) {
    const int piece = 8 * w + 4 * it + grp;
    const int d32 = piece >> 1, cb = piece & 1;
    const u32 a0 = smb + q * LN_RS + (32 * d32 + 16 * cb + 8 * (p & 1) + 4 * (p >> 1)) * 2;
    bf16x8 f = tr_frag(a0, a0 + 4 * LN_RS);
    __builtin_amdgcn_raw_buffer_store_b128(__builtin_bit_cast(u32x4, f), make_srd(hfrag, (u32)S * D * 2), (u32)((d32 * 128 + k16) * 64 + 32 * hh + 16 * cb + (lane & 15)) * 16u, 0, 16);
  }
  asm volatile("s_waitcnt vmcnt(0)" : "+v"(sink) :: "memory");

}

template <int NT, int NS, int KTOT, int LDB, bool SWAP>
struct Gemm {
  static constexpr int RB = NT * 64;
  static constexpr int SB = 64 * RB;
  static constexpr int GI = SB / 4096;
  static constexpr int CPR = RB / 16;
  static constexpr int RPI = 64 / CPR;
  static constexpr int PER = 4 + GI;
  static constexpr int NM = 2 * NT;
  static constexpr int NR = 4 * NT;
  static constexpr int NV = 2 + GI / 2;

  f32x16 acc[NT];
  bf16x8 dir[NS][2][2];
  s16x4 b0[NM][2], b1[NM][2];
  u32 vaddr[NT], vaddr2[NT];
  u32 vsrc[GI];
  u32 vdir;
  u32 sL, sD, m0base;
  __amdgpu_buffer_rsrc_t srdL, srdD;

  template <int SLOT, int KB, int I>
  __device__ __forceinline__ void rd(s16x4 (&b)[NM][2]) {
    constexpr int m = I >> 1, t = I & 1, ks = m / NT, ct = m % NT;
    if constexpr (SLOT * SB < 65536)
      b[m][t] = __builtin_amdgcn_ds_read_tr16_b64_v4i16((lds_s16x4p)(size_t)(vaddr[ct] + SLOT * SB + (KB * 32 + ks * 16 + t * 8) * RB));
    else
      b[m][t] = __builtin_amdgcn_ds_read_tr16_b64_v4i16((lds_s16x4p)(size_t)(vaddr2[ct] + (SLOT * SB - 65536) + (KB * 32 + ks * 16 + t * 8) * RB));
  }
  template <int SLOT, int KB, int I>
  __device__ __forceinline__ void mm(s16x4 (&b)[NM][2]) {
    constexpr int ks = I / NT, ct = I % NT;
    bf16x8 bb = __builtin_bit_cast(bf16x8, __builtin_shufflevector(b[I][0], b[I][1], 0, 1, 2, 3, 4, 5, 6, 7));
    if constexpr (SWAP) acc[ct] = __builtin_amdgcn_mfma_f32_32x32x16_bf16(bb, dir[SLOT][KB][ks], acc[ct], 0, 0, 0);
    else acc[ct] = __builtin_amdgcn_mfma_f32_32x32x16_bf16(dir[SLOT][KB][ks], bb, acc[ct], 0, 0, 0);
  }
  template <int SLOT, int KB, int T>
  __device__ __forceinline__ void loadD(int t) {
    ldfrag<KB * 2048 + T * 1024>(dir[SLOT][KB][T], vdir, srdD, sD + (u32)t * 4096u);
  }
  template <int SLOT, int I>
  __device__ __forceinline__ void dma(int t) {
    dma16i<I * 1024>(vsrc[I], srdL, sL + (u32)t * (64u * LDB));
  }
  template <int SLOT, int V>
  __device__ __forceinline__ void vmB(int t) {
    if constexpr (V == 0) set_m0(m0base + SLOT * SB);
    if constexpr (V < 2) loadD<SLOT, 0, V>(t);
    else dma<SLOT, V - 2>(t);
  }
  template <int SLOT, int V>
  __device__ __forceinline__ void vmA(int t) {
    if constexpr (V < GI / 2) dma<SLOT, GI / 2 + V>(t);
    else loadD<SLOT, 1, V - GI / 2>(t);
  }
  template <int SA, int SBn>
  __device__ __forceinline__ void tie() {
    asm volatile("" : "+v"(dir[SA][1][0]), "+v"(dir[SA][1][1]), "+v"(dir[SBn][0][0]), "+v"(dir[SBn][0][1]) :: "memory");
  }
#define RPSV 3
  static constexpr int RPS = RPSV;
  static constexpr int vslot(int I) {
    for (int v = 0; v < NV; v++)
      if (I == ((2 * v + 1) * NM) / (2 * NV)) return v;
    return -1;
  }
  template <int SLOT, int KB, int I, int J>
  __device__ __forceinline__ void rds(s16x4 (&b)[NM][2]) {
    if constexpr (J < RPS && RPS * I + J < NR) { rd<SLOT, KB, RPS * I + J>(b); rds<SLOT, KB, I, J + 1>(b); }
  }
  template <int Sl, bool ISSUE, int I>
  __device__ __forceinline__ void phaseA(int t) {
    constexpr int Pv = (Sl + NS - 1) % NS;
    mm<Sl, 0, I>(b0);
    __builtin_amdgcn_sched_barrier(0);
    rds<Sl, 1, I, 0>(b1);
    if constexpr (ISSUE && vslot(I) >= 0) vmA<Pv, vslot(I)>(t - 1 + NS);
    __builtin_amdgcn_sched_barrier(0);
    if constexpr (I + 1 < NM) phaseA<Sl, ISSUE, I + 1>(t);
  }
  template <int Sl, bool ISSUE, bool READ, int I>
  __device__ __forceinline__ void phaseB(int t) {
    constexpr int Nx = (Sl + 1) % NS;
    mm<Sl, 1, I>(b1);
    __builtin_amdgcn_sched_barrier(0);
    if constexpr (READ) rds<Nx, 0, I, 0>(b0);
    if constexpr (ISSUE && vslot(I) >= 0) vmB<Sl, vslot(I)>(t + NS);
    __builtin_amdgcn_sched_barrier(0);
    if constexpr (I + 1 < NM) phaseB<Sl, ISSUE, READ, I + 1>(t);
  }
  template <int Sl, bool TAIL, int EXTRA = 0>
  __device__ __forceinline__ void iter(int t) {
    constexpr int Nx = (Sl + 1) % NS;
    waitcnt<63, 0>();
    __builtin_amdgcn_sched_barrier(0);
    phaseA<Sl, (!TAIL || Sl == 0), 0>(t);
    constexpr int N0 = TAIL ? ((Sl <= NS - 2) ? (NS - 2 - Sl) * PER + 2 : 0) : (NS - 2) * PER + 2;
    constexpr int N = (TAIL && N0 >= NV) ? N0 + EXTRA : N0;
    asm volatile("" ::: "memory");
    waitcnt<N, 0>();
    tie<Sl, Nx>();
    constexpr bool LAST = TAIL && Sl == NS - 1;
    if constexpr (!LAST) __builtin_amdgcn_s_barrier();
    asm volatile("" ::: "memory");
    __builtin_amdgcn_sched_barrier(0);
    phaseB<Sl, !TAIL, !LAST, 0>(t);
  }
  template <int Sl, bool TAIL, int EXTRA = 0>
  __device__ __forceinline__ void group(int t) {
    iter<Sl, TAIL, EXTRA>(t + Sl);
    if constexpr (Sl + 1 < NS) group<Sl + 1, TAIL, EXTRA>(t);
  }
  template <int Sl, int V>
  __device__ __forceinline__ void proB() {
    vmB<Sl, V>(Sl);
    if constexpr (V + 1 < NV) proB<Sl, V + 1>();
  }
  template <int Sl, int V>
  __device__ __forceinline__ void proA() {
    vmA<Sl, V>(Sl);
    if constexpr (V + 1 < NV) proA<Sl, V + 1>();
  }
  template <int Sl>
  __device__ __forceinline__ void prologue() {
    proB<Sl, 0>();
    if constexpr (Sl + 1 < NS) { proA<Sl, 0>(); prologue<Sl + 1>(); }
  }
  template <int Sl>
  __device__ __forceinline__ void drain() {
    asm volatile("s_waitcnt vmcnt(0) lgkmcnt(0)" : "+v"(dir[Sl][0][0]), "+v"(dir[Sl][0][1]), "+v"(dir[Sl][1][0]), "+v"(dir[Sl][1][1]) :: "memory");
    if constexpr (Sl + 1 < NS) drain<Sl + 1>();
  }
  template <int I>
  __device__ __forceinline__ void rd0() {
    rd<0, 0, I>(b0);
    if constexpr (I + 1 < NR) rd0<I + 1>();
  }

  __device__ __forceinline__ void start(const u16* L, int n0, const u16* Dirp, int d32, u32 ldsbase, int t0, int nt) {
    const int lane = threadIdx.x & 63;
    const u32 w = rfl(threadIdx.x >> 6);
    srdL = make_srd(L, rfl((u32)(t0 + nt) * 64u * LDB));
    srdD = make_srd(Dirp + (size_t)rfl(d32) * (KTOT * 2048), rfl((u32)(t0 + nt) * 4096u));
    const int gm = lane >> 4, q = (lane >> 2) & 3, p = lane & 3, hh = lane >> 5;
#pragma unroll
    for (int c = 0; c < NT; c++) vaddr[c] = ldsbase + (4 * hh + q) * RB + ((c ^ (NT == 4 ? q : (q >> 1))) << 6) + 32 * (gm & 1) + 8 * p;
#pragma unroll
    for (int c = 0; c < NT; c++) { vaddr2[c] = vaddr[c] + 65536; asm volatile("" : "+v"(vaddr2[c])); }
    {
      const int rl = lane / CPR, cpos = lane % CPR, un = cpos >> 2, sub = cpos & 3;
#pragma unroll
      for (int i = 0; i < GI; i++) vsrc[i] = rl * LDB + (((un ^ (NT == 4 ? (rl & 3) : ((rl >> 1) & 1))) << 2) + sub) * 16 + i * (RPI * LDB - 1024);
    }
    sL = rfl((u32)n0 * 2u + w * 16u * LDB + (u32)t0 * (64u * LDB));
    m0base = rfl(ldsbase + w * (GI * 1024));
    vdir = lane * 16;
    sD = rfl((u32)t0 * 4096u);
#pragma unroll
    for (int s = 0; s < NS; s++)
#pragma unroll
      for (int k = 0; k < 2; k++)
#pragma unroll
        for (int t = 0; t < 2; t++) dir[s][k][t] = bf16x8{};
    asm volatile("s_nop 4" : "+s"(sL), "+s"(sD), "+s"(m0base), "+s"(srdL), "+s"(srdD) :: "memory");
    prologue<0>();
  }
  template <bool PEEL, int EXTRA, class Hook, class Hook2>
  __device__ __forceinline__ void main(int nt, Hook&& hook, Hook2&& hook2) {

    set_m0(m0base + (NS - 1) * SB);
    asm volatile("s_waitcnt vmcnt(%4)"
                 : "+v"(dir[0][0][0]), "+v"(dir[0][0][1]), "+v"(dir[0][1][0]), "+v"(dir[0][1][1])
                 : "i"((NS - 2) * PER + NV) : "memory");
    __builtin_amdgcn_s_barrier();
    asm volatile("" ::: "memory");
    rd0<0>();
    __builtin_amdgcn_sched_barrier(0);
    for (int t = 0; t < nt - (PEEL ? NS : 0); t += NS) { hook(t); group<0, false>(t); }
    if constexpr (PEEL) { hook2(); group<0, true, EXTRA>(nt - NS); }
    __builtin_amdgcn_sched_barrier(0);
    drain<0>();

  }
};

__device__ __forceinline__ f32x2 gelu2(f32x2 x) {
  const f32x2 k1 = {-2.302208198f, -2.302208198f}, k2 = {-0.1029432397f, -0.1029432397f}, one = {1.f, 1.f};
  f32x2 t = x * x;
  f32x2 z = x * __builtin_elementwise_fma(t, k2, k1);
  f32x2 e = {__builtin_amdgcn_exp2f(z[0]), __builtin_amdgcn_exp2f(z[1])};
  e = e + one;
  f32x2 r = {__builtin_amdgcn_rcpf(e[0]), __builtin_amdgcn_rcpf(e[1])};
  return x * r;
}

struct Conv {
  f32x4 acc[8];
  u32x4 Q[2][8];
  s16x4 P[2][8][2];
  u32 pr[2], pz[2], qr;
  u32x4 pm;
  int zl;
  template <int X, int J>
  __device__ __forceinline__ void rd() {
    if constexpr (J < 8) {
      constexpr int m = 7 - J;
      Q[X][m] = *(lds_u32x4p)(size_t)(qr + 512 * m + (X ? 0 : 64));
    } else {
      constexpr int e = (J - 8) >> 1, t = (J - 8) & 1;
      if constexpr (X == 0 && e == 0) P[X][e][t] = __builtin_amdgcn_ds_read_tr16_b64_v4i16((lds_s16x4p)(size_t)(pz[t]));
      else P[X][e][t] = __builtin_amdgcn_ds_read_tr16_b64_v4i16((lds_s16x4p)(size_t)(pr[t] + 2048 * e - (X ? 0 : 256)));
    }
  }
  static constexpr int me(int J, bool wantE) {
    int j = 0;
    for (int m = 7; m >= 0; --m)
      for (int e = 7 - m; e >= 0; --e) { if (j == J) return wantE ? e : m; ++j; }
    return 0;
  }
  template <int X, int J>
  __device__ __forceinline__ void mm() {
    constexpr int e = me(J, true), m = me(J, false);
    bf16x8 a = __builtin_bit_cast(bf16x8, __builtin_shufflevector(P[X][e][0], P[X][e][1], 0, 1, 2, 3, 4, 5, 6, 7));
    asm volatile("v_mfma_f32_16x16x32_bf16 %0, %1, %2, %0" : "+v"(acc[e + m]) : "v"(a), "v"(Q[X][m]));
  }
  template <int X, int J>
  __device__ __forceinline__ void slots(int thr) {
    mm<X, J>();
    __builtin_amdgcn_sched_barrier(0);
    if constexpr (J < 24) rd<X ^ 1, J>();
    if constexpr (J == 24) Q[X][0] = (zl < thr) ? u32x4{0, 0, 0, 0} : Q[X][0];
    __builtin_amdgcn_sched_barrier(0);
    if constexpr (J + 1 < 36) slots<X, J + 1>(thr);
  }
  template <int J>
  __device__ __forceinline__ void rd_first() {
    rd<0, J>();
    if constexpr (J + 1 < 24) rd_first<J + 1>();
  }
  __device__ __forceinline__ void run(float Dv, u32 hb, u32 xb) {
    const int lane = threadIdx.x & 63;
    const int g = lane >> 4, q = (lane >> 2) & 3, p = lane & 3, n = lane & 15;
#pragma unroll
    for (int t = 0; t < 2; t++) {
      const int I0 = 16 - 8 * g - 4 * t + 4 * p;
      pr[t] = hb + 256 + ((I0 >> 5) * 4 + q) * 64 + (I0 & 31) * 2;
      pz[t] = pr[t] - (I0 < 0 ? 0 : 256);
    }
    qr = xb + 32 * n - 32 + 16 * g - 64;
    asm volatile("" : "+v"(qr));
    zl = 2 * n + g;
    {
      int nv = n + 17 - 8 * g; nv = nv < 0 ? 0 : (nv > 8 ? 8 : nv);
#pragma unroll
      for (int wd = 0; wd < 4; wd++) {
        int bits = 16 * nv - 32 * wd;
        pm[wd] = bits >= 32 ? 0xffffffffu : (bits <= 0 ? 0u : 0xffffu);
      }
    }
    const u32 ya = xb + (16 * n + 4 * g) * 2;
#pragma unroll
    for (int i = 0; i < 8; i++) acc[i] = f32x4{0, 0, 0, 0};
    rd_first<0>();
    {
      u32x4 v = __builtin_bit_cast(u32x4, __builtin_shufflevector(P[0][0][0], P[0][0][1], 0, 1, 2, 3, 4, 5, 6, 7));
      v &= pm;
      s16x8 sv = __builtin_bit_cast(s16x8, v);
      P[0][0][0] = __builtin_shufflevector(sv, sv, 0, 1, 2, 3);
      P[0][0][1] = __builtin_shufflevector(sv, sv, 4, 5, 6, 7);
    }
    __builtin_amdgcn_sched_barrier(0);
#pragma unroll 1
    for (int de = 0; de < 8; de += 2) {
      waitcnt<63, 0>();
      asm volatile("s_nop 1");
      __builtin_amdgcn_sched_barrier(0);
      slots<0, 0>(4 * de + 2);
      pz[0] = pr[0] + 256; pz[1] = pr[1] + 256;
      pr[0] += 512; pr[1] += 512; qr -= 128;
      asm volatile("" : "+v"(qr), "+v"(pr[0]), "+v"(pr[1]));
      waitcnt<63, 0>();
      __builtin_amdgcn_sched_barrier(0);
      slots<1, 0>(4 * de + 6);
      __builtin_amdgcn_sched_barrier(0);
    }
    u32x2 kv[8];
#pragma unroll
    for (int i = 0; i < 8; i++) kv[i] = *(lds_u32x2p)(size_t)(ya + 512 * i);
    asm volatile("s_nop 7\n\ts_nop 1" : "+v"(acc[0]), "+v"(acc[1]), "+v"(acc[2]), "+v"(acc[3]), "+v"(acc[4]), "+v"(acc[5]), "+v"(acc[6]), "+v"(acc[7]));
#pragma unroll
    for (int i = 0; i < 8; i++) {
      float y0 = __builtin_fmaf(bflo(kv[i][0]), Dv, acc[i][0]);
      float y1 = __builtin_fmaf(bfhi(kv[i][0]), Dv, acc[i][1]);
      float y2 = __builtin_fmaf(bflo(kv[i][1]), Dv, acc[i][2]);
      float y3 = __builtin_fmaf(bfhi(kv[i][1]), Dv, acc[i][3]);
      *(lds_u32x2p)(size_t)(ya + 512 * i) = u32x2{pack2(y0, y1), pack2(y2, y3)};
    }
  }
};
__device__ __forceinline__ void conv_fill(int d, const u16* __restrict__ ssmk, const u16* __restrict__ kvT, u32 hb, u32 xb) {
  const int lane = threadIdx.x & 63;
  const u16* hrow = ssmk + (size_t)d * S;
  const u16* xrow = kvT + (size_t)d * S;
  u32x4 hv[4], xv[4];
  u32x2 pv[4];
#pragma unroll
  for (int i = 0; i < 4; i++) {
    const int e0 = (i * 64 + lane) * 8;
    hv[i] = *(const u32x4*)(hrow + e0);
    pv[i] = *(const u32x2*)(hrow + (e0 > 0 ? e0 - 4 : 0));
    xv[i] = *(const u32x4*)(xrow + e0);
  }
  __builtin_amdgcn_sched_barrier(0);
#pragma unroll
  for (int i = 0; i < 4; i++) {
    const int e0 = (i * 64 + lane) * 8;
    *(lds_u32x4p)(size_t)(xb + e0 * 2) = xv[i];
    const u32 a = hb + (e0 >> 5) * 256 + (e0 & 31) * 2;
    const u32 P0 = pv[i][0], P1 = pv[i][1], E0 = hv[i][0], E1 = hv[i][1], E2 = hv[i][2], E3 = hv[i][3];
    const u32 a10 = __builtin_amdgcn_alignbyte(P1, P0, 2), a0 = __builtin_amdgcn_alignbyte(E0, P1, 2),
              a1 = __builtin_amdgcn_alignbyte(E1, E0, 2), a2 = __builtin_amdgcn_alignbyte(E2, E1, 2),
              a3 = __builtin_amdgcn_alignbyte(E3, E2, 2);
    *(lds_u32x4p)(size_t)(a) = hv[i];
    *(lds_u32x4p)(size_t)(a + 64) = u32x4{a0, a1, a2, a3};
    *(lds_u32x4p)(size_t)(a + 128) = u32x4{P1, E0, E1, E2};
    *(lds_u32x4p)(size_t)(a + 192) = u32x4{a10, a0, a1, a2};
  }
}

#define NS1 4
static constexpr int MID_LDS = 81920;

__global__ __launch_bounds__(256, 2) void k_mid(const u16* __restrict__ hfrag, const u16* __restrict__ w1,
                                                const u16* __restrict__ b1, u16* __restrict__ ufrag,
                                                const u16* __restrict__ w2, const u16* __restrict__ ssmk,
                                                const u16* __restrict__ kvT, const u16* __restrict__ ssmD,
                                                u16* __restrict__ yp4) {
  extern __shared__ __attribute__((aligned(16))) char smem[];
  const u32 ldsbase = (u32)(size_t)(lds_charp)smem;
  const int lane = threadIdx.x & 63;
  const int w = rfl(threadIdx.x >> 6);
  const int xcd = blockIdx.x & 7, idx = blockIdx.x >> 3;

  if (idx < 28) {
    __builtin_amdgcn_s_setprio(3);
    const int mt = (xcd & 1) * 4 + (idx & 3), nt = (xcd >> 1) * 7 + (idx >> 2);
    const int d32 = mt * 4 + w, f0 = nt * 128;
    const int hh = lane >> 5;
    Gemm<4, NS1, S / 64, F * 2, true> gm;
    gm.start(w1, f0, hfrag, d32, ldsbase, 0, S / 64);
    {
      const u32* bp = (const u32*)(b1 + f0);
#pragma unroll
      for (int ct = 0; ct < 4; ct++)
#pragma unroll
        for (int a = 0; a < 4; a++) {
          const u32 l0 = bp[16 * ct + 4 * a], l1 = bp[16 * ct + 4 * a + 1], h0 = bp[16 * ct + 4 * a + 2], h1 = bp[16 * ct + 4 * a + 3];
          const u32 v0 = hh ? h0 : l0, v1 = hh ? h1 : l1;
          gm.acc[ct][4 * a] = bflo(v0); gm.acc[ct][4 * a + 1] = bfhi(v0); gm.acc[ct][4 * a + 2] = bflo(v1); gm.acc[ct][4 * a + 3] = bfhi(v1);
        }
    }
    gm.main<true, 0>(S / 64, [](int) {}, []() {});
#pragma unroll
    for (int kk = 0; kk < 8; kk++) {
      u32x4 ov;
#pragma unroll
      for (int j = 0; j < 4; j++) {
        f32x2 xx = {gm.acc[kk >> 1][8 * (kk & 1) + 2 * j], gm.acc[kk >> 1][8 * (kk & 1) + 2 * j + 1]};
        f32x2 o = gelu2(xx);
        ov[j] = pack2(o[0], o[1]);
      }
      __builtin_amdgcn_raw_buffer_store_b128(ov, make_srd(ufrag, (u32)D * F * 2), (u32)((d32 * 224 + (f0 >> 4) + kk) * 64 + lane) * 16u, 0, 16);
    }

  } else if (idx >= 32) {
    const int cb = blockIdx.x - 256;
    const int dA = cb * 4 + w;
    const float Dv = bf2f(ssmD[dA]);
    const u32 hb = ldsbase + w * 16384, xb = ldsbase + 65536 + w * 4096;
    conv_fill(dA, ssmk, kvT, hb, xb);

    { Conv cv; cv.run(Dv, hb, xb); }

    __syncthreads();
    const int grp = lane >> 4, q = (lane >> 2) & 3, p = lane & 3;
#pragma unroll
    for (int it = 0; it < 8; it++) {
      const int sb = 512 * w + 64 * it + 16 * grp;
      s16x4 v = __builtin_amdgcn_ds_read_tr16_b64_v4i16((lds_s16x4p)(size_t)(ldsbase + 65536 + q * 4096 + (sb + 4 * p) * 2));
      __builtin_amdgcn_raw_buffer_store_b64(__builtin_bit_cast(u32x2, v), make_srd(yp4, (u32)S * D * 2), (u32)(cb * S + 512 * w + 64 * it + lane) * 8u, 0, 16);
    }

  } else {
    const int gw = (blockIdx.x - 224) * 4 + w;
    u32 sink = 0;
    const u32 vt = lane * 128;

    __amdgpu_buffer_rsrc_t srd = make_srd(w1, (u32)S * F * 2), srd2 = make_srd(w2, (u32)S * F * 2);
    __builtin_amdgcn_s_sleep(64);
    for (u32 o = (PF0_WAVES + gw) * 8192u; o < (u32)S * F * 2; o += 128 * 8192u) touch(sink, vt, srd, o);
    for (u32 o = gw * 8192u; o < (u32)S * F * 2; o += 128 * 8192u) touch(sink, vt, srd2, o);
    asm volatile("s_waitcnt vmcnt(0)" : "+v"(sink) :: "memory");

  }
}

#define NS2 4
static constexpr int OUT_LDS = NS2 * 16384 > 98304 ? NS2 * 16384 : 98304;
#define RLX_AGENT __ATOMIC_RELAXED, __HIP_MEMORY_SCOPE_AGENT

template <int IMM>
__device__ __forceinline__ void ld16(u32x4& dst, u32 voff, __amdgpu_buffer_rsrc_t srd) {
  asm volatile("buffer_load_dwordx4 %0, %1, %2, 0 offen offset:%3" : "+v"(dst) : "v"(voff), "s"(srd), "i"(IMM) : "memory");
}
template <int IMM>
__device__ __forceinline__ void ld8(u32x2& dst, u32 voff, __amdgpu_buffer_rsrc_t srd) {
  asm volatile("buffer_load_dwordx2 %0, %1, %2, 0 offen offset:%3" : "+v"(dst) : "v"(voff), "s"(srd), "i"(IMM) : "memory");
}
__device__ __forceinline__ void spin_ge(gu32* word, u32 want) {
  for (u32 spins = 0; spins < (1u << 16); ++spins) {
    if (rfl(__hip_atomic_load(word, RLX_AGENT)) >= want) break;
    __builtin_amdgcn_s_sleep(1);
  }
  asm volatile("" ::: "memory");
}

__global__ __launch_bounds__(256, 1) void k_out(const u16* __restrict__ ufrag, const u16* __restrict__ w2,
                                                const u16* __restrict__ b2, const u16* __restrict__ yp4,
                                                float* part, u32* flags,
                                                const u16* __restrict__ x, const float2* __restrict__ stats,
                                                u16* __restrict__ out) {
  extern __shared__ __attribute__((aligned(16))) char smem[];
  const u32 ldsbase = (u32)(size_t)(lds_charp)smem;
  const int lane = threadIdx.x & 63;
  const int w = rfl(threadIdx.x >> 6);
  gu32* gflags = (gu32*)flags;

  const int xcd = blockIdx.x & 7, idx = blockIdx.x >> 3;
  const int half = xcd & 1;
  const int mt = ((xcd >> 1) & 1) * 4 + (idx & 3), nt = (xcd >> 2) * 8 + (idx >> 2);
  const int tile = mt * 16 + nt;
  const int d32 = mt * 4 + w, s0 = nt * 128;
  const int c = lane & 31, hh = lane >> 5;
  constexpr int KH = F / 128;
  Gemm<4, NS2, F / 64, S * 2, false> gm;
#pragma unroll
  for (int ct = 0; ct < 4; ct++)
#pragma unroll
    for (int r = 0; r < 16; r++) gm.acc[ct][r] = 0.f;
  gm.start(w2, s0, ufrag, d32, ldsbase, half * KH, KH);
  const int sk = s0 + 64 * half;
  u32x4 xv[4];
  u32x2 yv[8], st[2];
  u32 bs[2];
  auto hook2 = [&]() {
#pragma unroll
    for (int i = 0; i < 4; i++) xv[i] = u32x4{0, 0, 0, 0};
#pragma unroll
    for (int i = 0; i < 8; i++) yv[i] = u32x2{0, 0};
    st[0] = st[1] = u32x2{0, 0}; bs[0] = bs[1] = 0;
    __amdgpu_buffer_rsrc_t srdX = make_srd(x, (u32)S * D * 2), srdS = make_srd(stats, (u32)S * 8), srdB = make_srd(b2, (u32)S * 2),
                           srdY = make_srd(yp4, (u32)S * D * 2);
    const u32 vx = (u32)(sk + c) * (D * 2) + (d32 * 32 + 8 * hh) * 2;
    ld16<0>(xv[0], vx, srdX); ld16<32>(xv[1], vx, srdX);
    ld16<0>(xv[2], vx + 32 * D * 2, srdX); ld16<32>(xv[3], vx + 32 * D * 2, srdX);
    const u32 vy = (u32)(d32 * 8 + 2 * hh) * (S * 8) + (u32)(sk + c) * 8;
#pragma unroll
    for (int hf = 0; hf < 2; hf++)
#pragma unroll
      for (int e = 0; e < 2; e++) {
        ld8<0>(yv[hf * 2 + e], vy + (4 * hf + e) * (S * 8), srdY);
        ld8<256>(yv[4 + hf * 2 + e], vy + (4 * hf + e) * (S * 8), srdY);
      }
    ld8<0>(st[0], (u32)(sk + c) * 8u, srdS);
    ld8<256>(st[1], (u32)(sk + c) * 8u, srdS);
    asm volatile("buffer_load_ushort %0, %1, %2, 0 offen" : "+v"(bs[0]) : "v"((u32)(sk + c) * 2u), "s"(srdB) : "memory");
    asm volatile("buffer_load_ushort %0, %1, %2, 0 offen offset:64" : "+v"(bs[1]) : "v"((u32)(sk + c) * 2u), "s"(srdB) : "memory");
  };
  gm.main<true, 16>(KH, [](int) {}, hook2);
  asm volatile("" : "+v"(xv[0]), "+v"(xv[1]), "+v"(xv[2]), "+v"(xv[3]), "+v"(st[0]), "+v"(st[1]), "+v"(bs[0]), "+v"(bs[1]));
  asm volatile("" : "+v"(yv[0]), "+v"(yv[1]), "+v"(yv[2]), "+v"(yv[3]), "+v"(yv[4]), "+v"(yv[5]), "+v"(yv[6]), "+v"(yv[7]));
  f32x16 keep[2];
  {
    __amdgpu_buffer_rsrc_t srdP = make_srd(part, (u32)S * D * 4);
    const int me = (tile * 2 + half) * 4 + w, peer = (tile * 2 + (half ^ 1)) * 4 + w;
#pragma unroll
    for (int ct = 0; ct < 2; ct++) {
      const f32x16 snd = half ? gm.acc[ct] : gm.acc[2 + ct];
      keep[ct] = half ? gm.acc[2 + ct] : gm.acc[ct];
#pragma unroll
      for (int r8 = 0; r8 < 2; r8++) {
        u32x4 v = {pack2(snd[8 * r8], snd[8 * r8 + 1]), pack2(snd[8 * r8 + 2], snd[8 * r8 + 3]),
                   pack2(snd[8 * r8 + 4], snd[8 * r8 + 5]), pack2(snd[8 * r8 + 6], snd[8 * r8 + 7])};
        __builtin_amdgcn_raw_buffer_store_b128(v, srdP, (u32)me * 4096u + (u32)lane * 16u + (ct * 2 + r8) * 1024u, 0, 16);
      }
    }
    asm volatile("s_waitcnt vmcnt(0)" ::: "memory");
    if (lane == 0) __hip_atomic_store(gflags + me * FLAG_STRIDE, 1u, RLX_AGENT);
    spin_ge(gflags + peer * FLAG_STRIDE, 1u);
    u32x4 pp[4];
#pragma unroll
    for (int i = 0; i < 4; i++) pp[i] = __builtin_amdgcn_raw_buffer_load_b128(srdP, (u32)peer * 4096u + (u32)lane * 16u + i * 1024u, 0, 16);
    __builtin_amdgcn_sched_barrier(0);
#pragma unroll
    for (int i = 0; i < 4; i++)
#pragma unroll
      for (int r = 0; r < 4; r++) {
        keep[i >> 1][8 * (i & 1) + 2 * r] += bflo(pp[i][r]);
        keep[i >> 1][8 * (i & 1) + 2 * r + 1] += bfhi(pp[i][r]);
      }
  }
#pragma unroll
  for (int ct = 0; ct < 2; ct++) {
    const int s = sk + 32 * ct + c;
    const float m = __uint_as_float(st[ct][0]), rinv = __uint_as_float(st[ct][1]), bias = __uint_as_float(bs[ct] << 16);
#pragma unroll
    for (int hf = 0; hf < 2; hf++) {
      const int d = d32 * 32 + 16 * hf + 8 * hh;
      float o[8];
#pragma unroll
      for (int j = 0; j < 8; j++) {
        const u32 xw = xv[2 * ct + hf][j >> 1], yw = yv[4 * ct + 2 * hf + (j >> 2)][(j >> 1) & 1];
        const float xx = (j & 1) ? bfhi(xw) : bflo(xw);
        const float yy = (j & 1) ? bfhi(yw) : bflo(yw);
        const float xn = (xx - m) * rinv;
        o[j] = __builtin_fmaf(xn, yy, xx) + (keep[ct][8 * hf + j] + bias);
      }
      u32x4 ov = {pack2(o[0], o[1]), pack2(o[2], o[3]), pack2(o[4], o[5]), pack2(o[6], o[7])};
      *(u32x4*)(out + (size_t)s * D + d) = ov;
    }
  }

}

extern "C" void kernel_launch(void* const* d_in, const int* in_sizes, int n_in, void* d_out, int out_size,
                              void* d_ws, size_t ws_size, hipStream_t stream) {
  const u16 *q = (const u16*)d_in[0], *k = (const u16*)d_in[1], *v = (const u16*)d_in[2], *sh = (const u16*)d_in[3],
            *sk = (const u16*)d_in[4], *sD = (const u16*)d_in[5], *w1 = (const u16*)d_in[7], *b1 = (const u16*)d_in[8],
            *w2 = (const u16*)d_in[9], *b2 = (const u16*)d_in[10];
  char* ws = (char*)d_ws;
  u16* hfrag = (u16*)(ws + WS_HFRAG);
  u16* kvT = (u16*)(ws + WS_KVT);
  u16* yp8 = (u16*)(ws + WS_YP8);
  u16* ufrag = (u16*)(ws + WS_UFRAG);
  float2* stats = (float2*)(ws + WS_STATS);
  u32* flags = (u32*)(ws + WS_FLAGS);
  (void)hipFuncSetAttribute((const void*)k_mid, hipFuncAttributeMaxDynamicSharedMemorySize, MID_LDS);
  (void)hipFuncSetAttribute((const void*)k_out, hipFuncAttributeMaxDynamicSharedMemorySize, OUT_LDS);
  k_prep<<<256, 768, 0, stream>>>(q, w1, hfrag, stats, flags, k, v, sh, kvT);
  k_mid<<<512, 256, MID_LDS, stream>>>(hfrag, w1, b1, ufrag, w2, sk, kvT, sD, yp8);
  k_out<<<256, 256, OUT_LDS, stream>>>(ufrag, w2, b2, yp8, (float*)(ws + WS_PART), flags, q, stats, (u16*)d_out);
}
```
